```python
import torch

def _softmax(x, axis=-1):
    return torch.softmax(x, dim=axis)
def _gelu(x):
    return torch.nn.functional.gelu(x, approximate="tanh")
def _silu(x):
    return torch.nn.functional.silu(x)
def _sigmoid(x):
    return torch.sigmoid(x)
def _layernorm(x, axis=-1, eps=1e-5):
    m = x.mean(dim=axis, keepdim=True)
    v = x.var(dim=axis, keepdim=True, unbiased=False)
    return (x - m) / torch.sqrt(v + eps)
def _scatter_add(tgt, idx, src, axis=0):
    return tgt.clone().index_add_(axis, idx, src)
def _segment_sum(src, seg, n):
    out = torch.zeros((n,) + src.shape[1:], dtype=src.dtype)
    return out.index_add_(0, seg, src)
def _pad(x, pw):
    flat = tuple(v for pair in reversed(pw) for v in pair)
    return torch.nn.functional.pad(x, flat)

kernel_name = 'standalone__kv_cache_quant_int4_int2_kv_with_per_channel_or_per_token_scales__sampling'

def setup_inputs():

    _w = (lambda a: (lambda t: t.to(torch.bfloat16) if t.is_floating_point() else t)(torch.as_tensor(a)))
    B = 16
    Sq = 16
    Skv = 4096
    Hq = 32
    Hkv = 4
    Dh = 128
    bits = 2
    G = 32
    if G <= 0 or G >= Skv:
        G = max(1, Skv // 2)
    Skv = (Skv // G) * G
    ngk = Skv // G
    qmax = (1 << bits) - 1
    return {
        "q": _w((lambda *s: torch.randn(*s, dtype=torch.bfloat16) * 3.2)(B, Hq, Sq, Dh)),
        "qk": _w(torch.randint(0, qmax + 1, (B, Hkv, Skv, Dh)).to(torch.int32)),
        "qv": _w(torch.randint(0, qmax + 1, (B, Hkv, Skv, Dh)).to(torch.int32)),
        "k_scales": _w(torch.abs((lambda *s: torch.randn(*s, dtype=torch.bfloat16) * 3.2)(B, Hkv, ngk, Dh)) * 0.05 + 0.02),
        "k_zeros": _w((lambda *s: torch.randn(*s, dtype=torch.bfloat16) * 3.2)(B, Hkv, ngk, Dh) + float(qmax) / 2.0),
        "v_scales": _w(torch.abs((lambda *s: torch.randn(*s, dtype=torch.bfloat16) * 0.05)(B, Hkv, Skv, 1)) * 0.05 + 0.02),
        "v_zeros": _w((lambda *s: torch.randn(*s, dtype=torch.bfloat16) * 0.05)(B, Hkv, Skv, 1) + float(qmax) / 2.0),
    }

def reference(q, qk, qv, k_scales, k_zeros, v_scales, v_zeros):
    B, Hq, Sq, Dh = q.shape
    Hkv, Skv = qk.shape[1], qk.shape[2]
    ngk = k_scales.shape[2]
    G = Skv // ngk
    ks = (lambda x, n, axis: torch.repeat_interleave(x, n, dim=axis))(k_scales, G, axis=2)
    kz = (lambda x, n, axis: torch.repeat_interleave(x, n, dim=axis))(k_zeros, G, axis=2)
    k = ((lambda x, dt: x.to(dt))(qk, q.dtype) - kz) * ks
    v = ((lambda x, dt: x.to(dt))(qv, q.dtype) - v_zeros) * v_scales
    rep = Hq // Hkv
    if rep > 1:
        k = (lambda x, n, axis: torch.repeat_interleave(x, n, dim=axis))(k, rep, axis=1)
        v = (lambda x, n, axis: torch.repeat_interleave(x, n, dim=axis))(v, rep, axis=1)
    scale = float(Dh) ** -0.5
    logits = torch.einsum("bhqd,bhkd->bhqk", q, k) * scale
    p = _softmax(logits, axis=-1)
    o = torch.einsum("bhqk,bhkd->bhqd", p, v)
    return o
```

```cpp
#include <hip/hip_runtime.h>
#include <hip/hip_bf16.h>
#include <stdint.h>

#define PF_D 4
#define KV_AUX 2
#define ROUND_LOGITS 0

constexpr int B_ = 16, HQ = 32, HKV = 4, SQ = 16, SKV = 4096, DH = 128, GRP = 32;
constexpr int REP = HQ / HKV;
constexpr int ROWS = REP * SQ;
constexpr int NPAIR = B_ * HKV;
constexpr int NSPLIT = 4;
constexpr int KEYS = SKV / NSPLIT;
constexpr int TILE = 32;
constexpr int NT = KEYS / TILE;
constexpr int NGK = SKV / GRP;
static_assert(TILE == GRP, "tile == K quant group");
static_assert(PF_D % 2 == 0, "even prefetch depth");

constexpr int KROW = 272;
constexpr int KT_BYTES = TILE * KROW;
constexpr int VT_BYTES = TILE * DH * 2;
constexpr int SLOT = KT_BYTES + VT_BYTES;
constexpr int L_KS = 2 * SLOT;
constexpr int L_KZ = L_KS + NT * DH * 2;
constexpr int L_VS = L_KZ + NT * DH * 2;
constexpr int L_VZ = L_VS + KEYS * 2;
constexpr int LDS_BYTES = L_VZ + KEYS * 2;

constexpr size_t WS_PO = 0;
constexpr size_t PO_BYTES = (size_t)NPAIR * NSPLIT * ROWS * DH * 4;
constexpr size_t WS_PML = PO_BYTES;

typedef short bf16x8 __attribute__((ext_vector_type(8)));
typedef float f32x16 __attribute__((ext_vector_type(16)));
typedef float f32x4 __attribute__((ext_vector_type(4)));
typedef float f32x2 __attribute__((ext_vector_type(2)));
typedef unsigned u32x4 __attribute__((ext_vector_type(4)));
typedef unsigned u32x2 __attribute__((ext_vector_type(2)));
typedef __bf16 bf16x2_t __attribute__((ext_vector_type(2)));

__device__ __forceinline__ unsigned cvtpk(float lo, float hi) {
  f32x2 v = {lo, hi};
  bf16x2_t r = __builtin_convertvector(v, bf16x2_t);
  return __builtin_bit_cast(unsigned, r);
}
__device__ __forceinline__ float bflo(unsigned u) { return __uint_as_float(u << 16); }
__device__ __forceinline__ float bfhi(unsigned u) { return __uint_as_float(u & 0xffff0000u); }

constexpr float SCALE = 0.08838834764831845f;
constexpr float L2E = 1.4426950408889634f;

__device__ __forceinline__ void wg_barrier() {
  asm volatile("s_waitcnt lgkmcnt(0)" ::: "memory");
  __builtin_amdgcn_s_barrier();
  asm volatile("" ::: "memory");
  __builtin_amdgcn_sched_barrier(0);
}
#define PIN8(R) asm volatile("" : "+v"(R[0]), "+v"(R[1]), "+v"(R[2]), "+v"(R[3]), "+v"(R[4]), "+v"(R[5]), "+v"(R[6]), "+v"(R[7]))

__global__ __launch_bounds__(512, 2) void attn_split(const uint16_t* __restrict__ q, const int* __restrict__ qk, const int* __restrict__ qv,
                                                     const uint16_t* __restrict__ ksc, const uint16_t* __restrict__ kzr,
                                                     const uint16_t* __restrict__ vsc, const uint16_t* __restrict__ vzr,
                                                     float* __restrict__ po, float* __restrict__ pml) {
  __shared__ __attribute__((aligned(16))) char lds[LDS_BYTES];
  const int tid = threadIdx.x, lane = tid & 63;
  const int wave = __builtin_amdgcn_readfirstlane(tid >> 6);
  const int bid = blockIdx.x;
  const int pair = (bid & 7) + 8 * (bid >> 5), split = (bid >> 3) & 3;
  const int c = lane & 31, hh = lane >> 5;

  if (wave >= 4) {
    const int pw = wave - 4, isV = pw >> 1, u = pw & 1;
    const size_t off = ((size_t)pair * SKV + (size_t)split * KEYS) * DH;
    const int* base = (isV ? qv : qk) + off;
    const __amdgpu_buffer_rsrc_t rs = __builtin_amdgcn_make_buffer_rsrc((void*)base, 0, KEYS * DH * 4, 0x00020000);
    u32x4 raw[PF_D][8];
    if (!isV) {
      const int voff = u * 8192 + lane * 16;
#define KLOAD(J, T) do { _Pragma("unroll") for (int i = 0; i < 8; ++i) \
        raw[J][i] = __builtin_amdgcn_raw_buffer_load_b128(rs, voff + (T) * 16384 + i * 1024, 0, KV_AUX); } while (0)
#pragma unroll
      for (int j = 0; j < PF_D; ++j) KLOAD(j, j);
      wg_barrier();
      char* kw = lds + (16 * u + hh) * KROW + c * 8;
      const char* ksp = lds + L_KS + c * 8;
      for (int t0 = 0; t0 < NT; t0 += PF_D) {
#pragma unroll
        for (int j = 0; j < PF_D; ++j) {
          const int t = t0 + j;
          if (NT % PF_D != 0 && t >= NT) break;
          const u32x2 s2 = *(const u32x2*)(ksp + t * 256), z2 = *(const u32x2*)(ksp + (L_KZ - L_KS) + t * 256);
          const float s0 = bflo(s2[0]), s1 = bfhi(s2[0]), s2f = bflo(s2[1]), s3 = bfhi(s2[1]);
          const float z0 = bflo(z2[0]), z1 = bfhi(z2[0]), z2f = bflo(z2[1]), z3 = bfhi(z2[1]);
          char* dst = kw + (j & 1) * SLOT;
          PIN8(raw[j]);
#pragma unroll
          for (int i = 0; i < 8; ++i) {
            const u32x4 r = raw[j][i];
            const unsigned a = cvtpk((float)(int)r[0] - z0, (float)(int)r[1] - z1);
            const unsigned b = cvtpk((float)(int)r[2] - z2f, (float)(int)r[3] - z3);
            u32x2 o = {cvtpk(bflo(a) * s0, bfhi(a) * s1), cvtpk(bflo(b) * s2f, bfhi(b) * s3)};
            *(u32x2*)(dst + i * 2 * KROW) = o;
          }
          KLOAD(j, t + PF_D);
          wg_barrier();
        }
      }
#undef KLOAD
    } else {
      const int voff = u * 8192 + hh * 2048 + c * 16;
#define VLOAD(J, T) do { _Pragma("unroll") for (int i = 0; i < 8; ++i) \
        raw[J][i] = __builtin_amdgcn_raw_buffer_load_b128(rs, voff + (T) * 16384 + (i & 3) * 512 + (i >> 2) * 4096, 0, KV_AUX); } while (0)
#pragma unroll
      for (int j = 0; j < PF_D; ++j) VLOAD(j, j);
      wg_barrier();
      char* vw = lds + KT_BYTES + u * 4096 + lane * 16;
      const char* vsp = lds + L_VS + (16 * u + 4 * hh) * 2;
      for (int t0 = 0; t0 < NT; t0 += PF_D) {
#pragma unroll
        for (int j = 0; j < PF_D; ++j) {
          const int t = t0 + j;
          if (NT % PF_D != 0 && t >= NT) break;
          const u32x2 sa = *(const u32x2*)(vsp + t * 64), sb = *(const u32x2*)(vsp + t * 64 + 16);
          const u32x2 za = *(const u32x2*)(vsp + (L_VZ - L_VS) + t * 64), zb = *(const u32x2*)(vsp + (L_VZ - L_VS) + t * 64 + 16);
          const unsigned sw[4] = {sa[0], sa[1], sb[0], sb[1]}, zw[4] = {za[0], za[1], zb[0], zb[1]};
          char* dst = vw + (j & 1) * SLOT;
          PIN8(raw[j]);
          u32x4 fr[4];
#pragma unroll
          for (int ip = 0; ip < 4; ++ip) {
            const float sl = bflo(sw[ip]), sh = bfhi(sw[ip]), zl = bflo(zw[ip]), zh = bfhi(zw[ip]);
            const u32x4 ra = raw[j][2 * ip], rb = raw[j][2 * ip + 1];
#pragma unroll
            for (int e = 0; e < 4; ++e) {
              const unsigned a = cvtpk((float)(int)ra[e] - zl, (float)(int)rb[e] - zh);
              fr[e][ip] = cvtpk(bflo(a) * sl, bfhi(a) * sh);
            }
          }
#pragma unroll
          for (int e = 0; e < 4; ++e) *(u32x4*)(dst + e * 1024) = fr[e];
          VLOAD(j, t + PF_D);
          wg_barrier();
        }
      }
#undef VLOAD
    }
    return;
  }

  {
    const uint16_t* ks_g = ksc + ((size_t)pair * NGK + (size_t)split * NT) * DH;
    const uint16_t* kz_g = kzr + ((size_t)pair * NGK + (size_t)split * NT) * DH;
    const uint16_t* vv_g = (tid < 128 ? vsc : vzr) + (size_t)pair * SKV + (size_t)split * KEYS;
    const u32x4 a0 = *(const u32x4*)(ks_g + tid * 8), a1 = *(const u32x4*)(ks_g + 2048 + tid * 8);
    const u32x4 b0 = *(const u32x4*)(kz_g + tid * 8), b1 = *(const u32x4*)(kz_g + 2048 + tid * 8);
    const u32x4 v0 = *(const u32x4*)(vv_g + (tid & 127) * 8);
    *(u32x4*)(lds + L_KS + tid * 16) = a0; *(u32x4*)(lds + L_KS + 4096 + tid * 16) = a1;
    *(u32x4*)(lds + L_KZ + tid * 16) = b0; *(u32x4*)(lds + L_KZ + 4096 + tid * 16) = b1;
    *(u32x4*)(lds + L_VS + tid * 16) = v0;
  }
  bf16x8 qf[8];
  {
    const uint16_t* qrow = q + ((size_t)pair * ROWS + wave * 32 + c) * DH + hh * 8;
#pragma unroll
    for (int ks = 0; ks < 8; ++ks) qf[ks] = *(const bf16x8*)(qrow + ks * 16);
  }
  wg_barrier();
  f32x16 acc[4] = {};
  float m = -2.0e38f, l = 0.f;
  const char* kr = lds + c * KROW + hh * 16;
  const char* vr = lds + KT_BYTES + lane * 16;
  for (int t0 = 0; t0 < NT; t0 += 2) {
#pragma unroll
    for (int j = 0; j < 2; ++j) {
      wg_barrier();
      bf16x8 kf[8], vf[8];
#pragma unroll
      for (int ks = 0; ks < 8; ++ks) kf[ks] = *(const bf16x8*)(kr + j * SLOT + ks * 32);
#pragma unroll
      for (int i = 0; i < 8; ++i) vf[i] = *(const bf16x8*)(vr + j * SLOT + i * 1024);
      f32x16 s = {};
#pragma unroll
      for (int ks = 0; ks < 8; ++ks) s = __builtin_amdgcn_mfma_f32_32x32x16_bf16(kf[ks], qf[ks], s, 0, 0, 0);
      float x[16];
#pragma unroll
      for (int r = 0; r < 16; ++r) x[r] = s[r] * SCALE;
      float mx = x[0];
#pragma unroll
      for (int r = 1; r < 16; ++r) mx = fmaxf(mx, x[r]);
      {
        auto rr = __builtin_amdgcn_permlane32_swap(__float_as_uint(mx), __float_as_uint(mx), false, false);
        mx = fmaxf(__uint_as_float(rr[0]), __uint_as_float(rr[1]));
      }
      const float mn = fmaxf(m, mx);
      const float alpha = __builtin_amdgcn_exp2f((m - mn) * L2E);
      m = mn;
      const float nm = -mn * L2E;
      float ps = 0.f;
#pragma unroll
      for (int r = 0; r < 16; ++r) { x[r] = __builtin_amdgcn_exp2f(fmaf(x[r], L2E, nm)); ps += x[r]; }
      l = l * alpha + ps;
      if (__any(alpha != 1.f)) {
#pragma unroll
        for (int d = 0; d < 4; ++d)
#pragma unroll
          for (int r = 0; r < 16; ++r) acc[d][r] *= alpha;
      }
      u32x4 p0 = {cvtpk(x[0], x[1]), cvtpk(x[2], x[3]), cvtpk(x[4], x[5]), cvtpk(x[6], x[7])};
      u32x4 p1 = {cvtpk(x[8], x[9]), cvtpk(x[10], x[11]), cvtpk(x[12], x[13]), cvtpk(x[14], x[15])};
      const bf16x8 pf0 = __builtin_bit_cast(bf16x8, p0), pf1 = __builtin_bit_cast(bf16x8, p1);
#pragma unroll
      for (int d = 0; d < 4; ++d) acc[d] = __builtin_amdgcn_mfma_f32_32x32x16_bf16(vf[d], pf0, acc[d], 0, 0, 0);
#pragma unroll
      for (int d = 0; d < 4; ++d) acc[d] = __builtin_amdgcn_mfma_f32_32x32x16_bf16(vf[4 + d], pf1, acc[d], 0, 0, 0);
    }
  }
  {
    auto rr = __builtin_amdgcn_permlane32_swap(__float_as_uint(l), __float_as_uint(l), false, false);
    l = __uint_as_float(rr[0]) + __uint_as_float(rr[1]);
  }
  float* pob = po + ((size_t)(pair * NSPLIT + split) * 4) * 4096 + wave * 1024 + lane * 4;
#pragma unroll
  for (int g = 0; g < 4; ++g)
#pragma unroll
    for (int j = 0; j < 4; ++j) {
      f32x4 v = {acc[0][4 * g + j], acc[1][4 * g + j], acc[2][4 * g + j], acc[3][4 * g + j]};
      *(f32x4*)(pob + g * 4096 + j * 256) = v;
    }
  if (hh == 0) {
    f32x2 ml = {m * L2E, l};
    *(f32x2*)(pml + ((size_t)(pair * NSPLIT + split) * ROWS + wave * 32 + c) * 2) = ml;
  }
}

__global__ __launch_bounds__(256) void attn_combine(const float* __restrict__ po, const float* __restrict__ pml, uint16_t* __restrict__ out) {
  const int tid = threadIdx.x, lane = tid & 63, wave = tid >> 6, c = lane & 31, hh = lane >> 5;
  const int bid = blockIdx.x;
  const int pair = (bid & 7) + 8 * (bid >> 5), g = (bid >> 3) & 3;
  f32x4 v[4][4]; f32x2 ml[4];
#pragma unroll
  for (int s = 0; s < 4; ++s) {
    ml[s] = *(const f32x2*)(pml + ((size_t)(pair * NSPLIT + s) * ROWS + wave * 32 + c) * 2);
    const float* p = po + ((size_t)(pair * NSPLIT + s) * 4 + g) * 4096 + wave * 1024 + lane * 4;
#pragma unroll
    for (int j = 0; j < 4; ++j) v[s][j] = *(const f32x4*)(p + j * 256);
  }
  const float M = fmaxf(fmaxf(ml[0][0], ml[1][0]), fmaxf(ml[2][0], ml[3][0]));
  float w[4], L = 0.f;
#pragma unroll
  for (int s = 0; s < 4; ++s) { w[s] = __builtin_amdgcn_exp2f(ml[s][0] - M); L += w[s] * ml[s][1]; }
  const float inv = 1.f / L;
  unsigned o[8];
#pragma unroll
  for (int j = 0; j < 4; ++j) {
    float r[4];
#pragma unroll
    for (int e = 0; e < 4; ++e) r[e] = (w[0] * v[0][j][e] + w[1] * v[1][j][e] + w[2] * v[2][j][e] + w[3] * v[3][j][e]) * inv;
    o[2 * j] = cvtpk(r[0], r[1]); o[2 * j + 1] = cvtpk(r[2], r[3]);
  }
  uint16_t* orow = out + ((size_t)pair * ROWS + wave * 32 + c) * DH + 32 * g + 16 * hh;
  *(u32x4*)orow = u32x4{o[0], o[1], o[2], o[3]};
  *(u32x4*)(orow + 8) = u32x4{o[4], o[5], o[6], o[7]};
}

extern "C" void kernel_launch(void* const* d_in, const int* in_sizes, int n_in, void* d_out, int out_size, void* d_ws, size_t ws_size,
                              hipStream_t stream) {
  float* po = (float*)((char*)d_ws + WS_PO);
  float* pml = (float*)((char*)d_ws + WS_PML);
  hipLaunchKernelGGL(attn_split, dim3(NPAIR * NSPLIT), dim3(512), 0, stream, (const uint16_t*)d_in[0], (const int*)d_in[1], (const int*)d_in[2],
                     (const uint16_t*)d_in[3], (const uint16_t*)d_in[4], (const uint16_t*)d_in[5], (const uint16_t*)d_in[6], po, pml);
  hipLaunchKernelGGL(attn_combine, dim3(NPAIR * 4), dim3(256), 0, stream, (const float*)po, (const float*)pml, (uint16_t*)d_out);
}
```

```cpp
#include <hip/hip_runtime.h>
#include <hip/hip_bf16.h>
#include <stdint.h>
#include <type_traits>

#define RING 2
#define KV_AUX 2
#define RESCALE_THR 6.0f
#define FUSED 1
#define PROD_PRIO 2

constexpr int B_ = 16, HQ = 32, HKV = 4, SQ = 16, SKV = 4096, DH = 128, GRP = 32;
constexpr int REP = HQ / HKV;
constexpr int ROWS = REP * SQ;
constexpr int NPAIR = B_ * HKV;
constexpr int NSPLIT = 4;
constexpr int KEYS = SKV / NSPLIT;
constexpr int TILE = 32;
constexpr int NT = KEYS / TILE;
constexpr int NGK = SKV / GRP;
static_assert(TILE == GRP, "tile == K quant group");

constexpr int KBLK = 272;
constexpr int KT_BYTES = 32 * KBLK;
constexpr int VROW = 320;
constexpr int VT_BYTES = TILE * VROW;
constexpr int SLOT = KT_BYTES + VT_BYTES;
constexpr int L_KTAB = 2 * SLOT;
constexpr int L_VTAB = L_KTAB + NT * 1024;
constexpr int LDS_BYTES = L_VTAB + KEYS * 8;

constexpr size_t WS_E = 0;
constexpr size_t WS_F = 16384;
constexpr size_t WS_PML = 65536;
constexpr size_t WS_PO = WS_PML + (size_t)NPAIR * NSPLIT * ROWS * 8;
constexpr int PO_PAIR = NSPLIT * 4 * 4 * 4096;

typedef short bf16x8 __attribute__((ext_vector_type(8)));
typedef short s16x4 __attribute__((ext_vector_type(4)));
typedef float f32x16 __attribute__((ext_vector_type(16)));
typedef float f32x4 __attribute__((ext_vector_type(4)));
typedef float f32x2 __attribute__((ext_vector_type(2)));
typedef unsigned u32x4 __attribute__((ext_vector_type(4)));
typedef unsigned u32x2 __attribute__((ext_vector_type(2)));
typedef __bf16 bf16x2_t __attribute__((ext_vector_type(2)));
typedef __attribute__((address_space(3))) s16x4* lds_s16x4p;
typedef __attribute__((address_space(1))) unsigned long long gu64;

__device__ __forceinline__ unsigned cvtpk(float lo, float hi) {
  f32x2 v = {lo, hi};
  bf16x2_t r = __builtin_convertvector(v, bf16x2_t);
  return __builtin_bit_cast(unsigned, r);
}
__device__ __forceinline__ float bflo(unsigned u) { return __uint_as_float(u << 16); }
__device__ __forceinline__ float bfhi(unsigned u) { return __uint_as_float(u & 0xffff0000u); }
__device__ __forceinline__ unsigned pku16(unsigned a, unsigned b) {
  return __builtin_bit_cast(unsigned, __builtin_amdgcn_cvt_pk_u16(a, b));
}
__device__ __forceinline__ unsigned deq2(float qa, float za, float sa, float qb, float zb, float sb) {
  const unsigned a = cvtpk(qa - za, qb - zb);
  return cvtpk(bflo(a) * sa, bfhi(a) * sb);
}
__device__ __forceinline__ unsigned lut2(unsigned t, unsigned tlo, unsigned thi) {
  return __builtin_amdgcn_perm(thi, tlo, __umul24(t, 0x0202u) + 0x01000100u);
}
typedef __attribute__((address_space(3))) char lds_char;
__device__ __forceinline__ bf16x8 trread2(lds_char* p0, lds_char* p1) {
  const s16x4 a = __builtin_amdgcn_ds_read_tr16_b64_v4i16((lds_s16x4p)p0);
  const s16x4 b = __builtin_amdgcn_ds_read_tr16_b64_v4i16((lds_s16x4p)p1);
  return __builtin_shufflevector(a, b, 0, 1, 2, 3, 4, 5, 6, 7);
}

constexpr float SCALE = 0.08838834764831845f;
constexpr float L2E = 1.4426950408889634f;

__device__ __forceinline__ void wg_barrier() {
  asm volatile("s_waitcnt lgkmcnt(0)" ::: "memory");
  __builtin_amdgcn_s_barrier();
  asm volatile("" ::: "memory");
  __builtin_amdgcn_sched_barrier(0);
}
#define PIN8(R) asm volatile("" : "+v"(R[0]), "+v"(R[1]), "+v"(R[2]), "+v"(R[3]), "+v"(R[4]), "+v"(R[5]), "+v"(R[6]), "+v"(R[7]))

__global__ __launch_bounds__(512, 2) void attn_split(const uint16_t* __restrict__ q, const int* __restrict__ qk, const int* __restrict__ qv,
                                                     const uint16_t* __restrict__ ksc, const uint16_t* __restrict__ kzr,
                                                     const uint16_t* __restrict__ vsc, const uint16_t* __restrict__ vzr,
                                                     char* __restrict__ ws, uint16_t* __restrict__ out) {
  __shared__ __attribute__((aligned(16))) char lds[LDS_BYTES];
  const int tid = threadIdx.x, lane = tid & 63;
  const int wave = __builtin_amdgcn_readfirstlane(tid >> 6);
  const int bid = blockIdx.x;
  const int pair = (bid & 7) + 8 * (bid >> 5), split = (bid >> 3) & 3;
  const int c = lane & 31, hh = lane >> 5;

  const size_t kso = ((size_t)pair * NGK + (size_t)split * NT) * DH;
  const size_t vso = (size_t)pair * SKV + (size_t)split * KEYS;
  u32x4 ks8 = *(const u32x4*)(ksc + kso + tid * 8), kz8 = *(const u32x4*)(kzr + kso + tid * 8);
  unsigned vs2 = *(const unsigned*)(vsc + vso + tid * 2), vz2 = *(const unsigned*)(vzr + vso + tid * 2);

  auto build_tables = [&]() {
    asm volatile("" : "+v"(ks8), "+v"(kz8), "+v"(vs2), "+v"(vz2));
    char* kt = lds + L_KTAB + tid * 64;
#pragma unroll
    for (int w = 0; w < 4; ++w) {
      const float sl = bflo(ks8[w]), sh = bfhi(ks8[w]), zl = bflo(kz8[w]), zh = bfhi(kz8[w]);
      const u32x4 e = {deq2(0.f, zl, sl, 1.f, zl, sl), deq2(2.f, zl, sl, 3.f, zl, sl), deq2(0.f, zh, sh, 1.f, zh, sh), deq2(2.f, zh, sh, 3.f, zh, sh)};
      *(u32x4*)(kt + w * 16) = e;
    }
    const float sl = bflo(vs2), sh = bfhi(vs2), zl = bflo(vz2), zh = bfhi(vz2);
    const u32x4 e = {deq2(0.f, zl, sl, 1.f, zl, sl), deq2(2.f, zl, sl, 3.f, zl, sl), deq2(0.f, zh, sh, 1.f, zh, sh), deq2(2.f, zh, sh, 3.f, zh, sh)};
    *(u32x4*)(lds + L_VTAB + tid * 16) = e;
  };

  if (wave >= 4) {
    if (PROD_PRIO) __builtin_amdgcn_s_setprio(PROD_PRIO);
    const int pw = wave - 4, isV = pw >> 1, u = pw & 1;
    const size_t off = ((size_t)pair * SKV + (size_t)split * KEYS) * DH;
    const int* base = (isV ? qv : qk) + off;
    const __amdgpu_buffer_rsrc_t rs = __builtin_amdgcn_make_buffer_rsrc((void*)base, 0, KEYS * DH * 4, 0x00020000);
    const int krow = 16 * u + 8 * hh;
    const int voff = krow * 512 + c * 16;
    u32x4 raw[RING][8];
#define LOADT(J, T) do { int vo_ = voff + (T) * 16384; asm volatile("" : "+v"(vo_)); _Pragma("unroll") for (int i = 0; i < 8; ++i) \
      raw[J][i] = __builtin_amdgcn_raw_buffer_load_b128(rs, vo_ + i * 512, 0, KV_AUX); } while (0)
    __builtin_amdgcn_sched_barrier(0);
    __builtin_amdgcn_s_barrier();
    __builtin_amdgcn_sched_barrier(0);
#pragma unroll
    for (int j = 0; j < RING - 1; ++j) LOADT(j, j);
    __builtin_amdgcn_sched_barrier(0);
    build_tables();
    wg_barrier();

    char* const wr = isV ? lds + KT_BYTES + krow * VROW + c * 8 : lds + c * KBLK + (2 * u + hh) * 16;
    const char* const tb = isV ? lds + L_VTAB + krow * 8 : lds + L_KTAB + c * 32;
    const int tstride = isV ? 256 : 1024;
    auto step = [&](auto jc, int t) {
      constexpr int J = decltype(jc)::value;
      char* dst = wr + (t & 1) * SLOT;
      u32x4 tv[4];
#pragma unroll
      for (int w = 0; w < 4; ++w) tv[w] = *(const u32x4*)(tb + t * tstride + w * 16);
      PIN8(raw[J]);
      LOADT((J + RING - 1) % RING, t + RING - 1);
      __builtin_amdgcn_sched_barrier(0);
      if (!isV) {
        unsigned tp[4][4], bad = 0;
#pragma unroll
        for (int e = 0; e < 4; ++e)
#pragma unroll
          for (int ip = 0; ip < 4; ++ip) { tp[e][ip] = pku16(raw[J][2 * ip][e], raw[J][2 * ip + 1][e]); bad |= tp[e][ip]; }
        u32x4 o[4];
        if (__builtin_expect(__any((bad & 0xfffcfffcu) != 0), 0)) {
          const u32x2 s2 = *(const u32x2*)(ksc + kso + t * DH + 4 * c), z2 = *(const u32x2*)(kzr + kso + t * DH + 4 * c);
          const float sf[4] = {bflo(s2[0]), bfhi(s2[0]), bflo(s2[1]), bfhi(s2[1])}, zf[4] = {bflo(z2[0]), bfhi(z2[0]), bflo(z2[1]), bfhi(z2[1])};
#pragma unroll
          for (int e = 0; e < 4; ++e)
#pragma unroll
            for (int ip = 0; ip < 4; ++ip) {
              const unsigned qq = cvtpk((float)(int)raw[J][2 * ip][e], (float)(int)raw[J][2 * ip + 1][e]);
              o[e][ip] = deq2(bflo(qq), zf[e], sf[e], bfhi(qq), zf[e], sf[e]);
            }
        } else {
          const unsigned tl[4] = {tv[0][0], tv[0][2], tv[1][0], tv[1][2]}, th[4] = {tv[0][1], tv[0][3], tv[1][1], tv[1][3]};
#pragma unroll
          for (int e = 0; e < 4; ++e)
#pragma unroll
            for (int ip = 0; ip < 4; ++ip) o[e][ip] = lut2(tp[e][ip], tl[e], th[e]);
        }
#pragma unroll
        for (int e = 0; e < 4; ++e) *(u32x4*)(dst + e * 64) = o[e];
      } else {
        unsigned tp[8][2], bad = 0;
#pragma unroll
        for (int i = 0; i < 8; ++i) {
          tp[i][0] = pku16(raw[J][i][0], raw[J][i][1]); tp[i][1] = pku16(raw[J][i][2], raw[J][i][3]);
          bad |= tp[i][0] | tp[i][1];
        }
        u32x2 o[8];
        if (__builtin_expect(__any((bad & 0xfffcfffcu) != 0), 0)) {
          const u32x4 s8 = *(const u32x4*)(vsc + vso + t * TILE + krow), z8 = *(const u32x4*)(vzr + vso + t * TILE + krow);
#pragma unroll
          for (int i = 0; i < 8; ++i) {
            const float sf = (i & 1) ? bfhi(s8[i >> 1]) : bflo(s8[i >> 1]), zf = (i & 1) ? bfhi(z8[i >> 1]) : bflo(z8[i >> 1]);
            const unsigned qa = cvtpk((float)(int)raw[J][i][0], (float)(int)raw[J][i][1]), qb = cvtpk((float)(int)raw[J][i][2], (float)(int)raw[J][i][3]);
            o[i][0] = deq2(bflo(qa), zf, sf, bfhi(qa), zf, sf); o[i][1] = deq2(bflo(qb), zf, sf, bfhi(qb), zf, sf);
          }
        } else {
#pragma unroll
          for (int i = 0; i < 8; ++i) {
            const unsigned tl = tv[i >> 1][2 * (i & 1)], th = tv[i >> 1][2 * (i & 1) + 1];
            o[i][0] = lut2(tp[i][0], tl, th); o[i][1] = lut2(tp[i][1], tl, th);
          }
        }
#pragma unroll
        for (int i = 0; i < 8; ++i) *(u32x2*)(dst + i * VROW) = o[i];
      }
      wg_barrier();
    };
    int t = 0;
    for (; t + RING <= NT; t += RING) {
      step(std::integral_constant<int, 0>{}, t);
      if constexpr (RING > 1) step(std::integral_constant<int, 1 % RING>{}, t + 1);
      if constexpr (RING > 2) step(std::integral_constant<int, 2 % RING>{}, t + 2);
      if constexpr (RING > 3) step(std::integral_constant<int, 3 % RING>{}, t + 3);
      if constexpr (RING > 4) step(std::integral_constant<int, 4 % RING>{}, t + 4);
    }
    if constexpr (NT % RING > 0) step(std::integral_constant<int, 0>{}, NT - NT % RING);
    if constexpr (NT % RING > 1) step(std::integral_constant<int, 1 % RING>{}, NT - NT % RING + 1);
    if constexpr (NT % RING > 2) step(std::integral_constant<int, 2 % RING>{}, NT - NT % RING + 2);
#undef LOADT
    __builtin_amdgcn_s_barrier();
    return;
  }

  bf16x8 qf[8];
  {
    const uint16_t* qrow = q + ((size_t)pair * ROWS + wave * 32 + c) * DH + hh * 8;
#pragma unroll
    for (int ks = 0; ks < 8; ++ks) qf[ks] = *(const bf16x8*)(qrow + ks * 16);
  }
  gu64* ep = (gu64*)(ws + WS_E + (size_t)pair * 256);
  const unsigned long long epoch0 = __hip_atomic_load(ep, __ATOMIC_RELAXED, __HIP_MEMORY_SCOPE_AGENT);
  __builtin_amdgcn_sched_barrier(0);
  __builtin_amdgcn_s_barrier();
  __builtin_amdgcn_sched_barrier(0);
  build_tables();
  wg_barrier();
  f32x16 acc[4] = {};
  float m = -2.0e38f, l = 0.f;
  const int rq = (lane & 15) >> 2, rp = lane & 3, rb = (lane >> 4) & 1;
  lds_char* kr = (lds_char*)lds + 2 * hh * KBLK + rq * 64 + rb * 32 + rp * 8;
  lds_char* vr = (lds_char*)lds + KT_BYTES + (4 * hh + rq) * VROW + rb * 32 + rp * 8;
  for (int t0 = 0; t0 < NT; t0 += 2) {
#pragma unroll
    for (int j = 0; j < 2; ++j) {
      wg_barrier();
      bf16x8 kf[8], vf[8];
#pragma unroll
      for (int ks = 0; ks < 8; ++ks) kf[ks] = trread2(kr + j * SLOT + (4 * ks) * KBLK, kr + j * SLOT + (4 * ks + 1) * KBLK);
      __builtin_amdgcn_sched_barrier(0);
      f32x16 s = {};
#pragma unroll
      for (int ks = 0; ks < 8; ++ks) {
        s = __builtin_amdgcn_mfma_f32_32x32x16_bf16(kf[ks], qf[ks], s, 0, 0, 0);
        const int s2 = ks >> 2, db = ks & 3;
        vf[ks] = trread2(vr + j * SLOT + (16 * s2) * VROW + db * 64, vr + j * SLOT + (16 * s2 + 8) * VROW + db * 64);
        __builtin_amdgcn_sched_barrier(0);
      }
      float x[16];
#pragma unroll
      for (int r = 0; r < 16; r += 2) {
        const unsigned a = cvtpk(s[r], s[r + 1]);
        const unsigned b = cvtpk(bflo(a) * SCALE, bfhi(a) * SCALE);
        x[r] = bflo(b); x[r + 1] = bfhi(b);
      }
      float mx = x[0];
#pragma unroll
      for (int r = 1; r < 16; ++r) mx = fmaxf(mx, x[r]);
      {
        auto rr = __builtin_amdgcn_permlane32_swap(__float_as_uint(mx), __float_as_uint(mx), false, false);
        mx = fmaxf(__uint_as_float(rr[0]), __uint_as_float(rr[1]));
      }
      if (__any(!(mx - m <= RESCALE_THR))) {
        const float mn = fmaxf(m, mx);
        const float alpha = __builtin_amdgcn_exp2f((m - mn) * L2E);
        m = mn;
        l *= alpha;
#pragma unroll
        for (int d = 0; d < 4; ++d)
#pragma unroll
          for (int r = 0; r < 16; ++r) acc[d][r] *= alpha;
      }
      const float nm = -m * L2E;
      float ps = 0.f;
#pragma unroll
      for (int r = 0; r < 16; ++r) { x[r] = __builtin_amdgcn_exp2f(fmaf(x[r], L2E, nm)); ps += x[r]; }
      l += ps;
      u32x4 p0 = {cvtpk(x[0], x[1]), cvtpk(x[2], x[3]), cvtpk(x[4], x[5]), cvtpk(x[6], x[7])};
      u32x4 p1 = {cvtpk(x[8], x[9]), cvtpk(x[10], x[11]), cvtpk(x[12], x[13]), cvtpk(x[14], x[15])};
      const bf16x8 pf0 = __builtin_bit_cast(bf16x8, p0), pf1 = __builtin_bit_cast(bf16x8, p1);
#pragma unroll
      for (int d = 0; d < 4; ++d) acc[d] = __builtin_amdgcn_mfma_f32_32x32x16_bf16(vf[d], pf0, acc[d], 0, 0, 0);
#pragma unroll
      for (int d = 0; d < 4; ++d) acc[d] = __builtin_amdgcn_mfma_f32_32x32x16_bf16(vf[4 + d], pf1, acc[d], 0, 0, 0);
    }
  }
  {
    auto rr = __builtin_amdgcn_permlane32_swap(__float_as_uint(l), __float_as_uint(l), false, false);
    l = __uint_as_float(rr[0]) + __uint_as_float(rr[1]);
  }
  const float mL = m * L2E;
  const __amdgpu_buffer_rsrc_t rpo = __builtin_amdgcn_make_buffer_rsrc((void*)(ws + WS_PO + (size_t)pair * PO_PAIR), 0, PO_PAIR, 0x00020000);
  const __amdgpu_buffer_rsrc_t rml = __builtin_amdgcn_make_buffer_rsrc((void*)(ws + WS_PML + (size_t)pair * (NSPLIT * ROWS * 8)), 0, NSPLIT * ROWS * 8, 0x00020000);
  constexpr int PAUX = FUSED ? 16 : 0;
  const int pvo = wave * 4096 + lane * 16;
#pragma unroll
  for (int db = 0; db < 4; ++db) {
    if (FUSED && db == split) continue;
#pragma unroll
    for (int j = 0; j < 4; ++j) {
      const u32x4 v = {__float_as_uint(acc[db][4 * j]), __float_as_uint(acc[db][4 * j + 1]), __float_as_uint(acc[db][4 * j + 2]), __float_as_uint(acc[db][4 * j + 3])};
      __builtin_amdgcn_raw_buffer_store_b128(v, rpo, pvo + j * 1024, (split * 4 + db) * 16384, PAUX);
    }
  }
  if (hh == 0) {
    const u32x2 ml = {__float_as_uint(mL), __float_as_uint(l)};
    __builtin_amdgcn_raw_buffer_store_b64(ml, rml, (wave * 32 + c) * 8, split * (ROWS * 8), PAUX);
  }
  const unsigned long long tag = epoch0 + 1;
  gu64* fl = (gu64*)(ws + WS_F + (size_t)pair * 512);
  asm volatile("s_waitcnt vmcnt(0)" ::: "memory");
  __builtin_amdgcn_s_barrier();
  asm volatile("" ::: "memory");
  if (tid == 0) __hip_atomic_store(fl + split * 16, tag, __ATOMIC_RELAXED, __HIP_MEMORY_SCOPE_AGENT);
  f32x16 own;
  if (split == 0) own = acc[0]; else if (split == 1) own = acc[1]; else if (split == 2) own = acc[2]; else own = acc[3];
  for (unsigned spins = 0; spins < (1u << 20); ++spins) {
    unsigned long long f = tag;
    if (lane < 4 && lane != split) f = __hip_atomic_load(fl + lane * 16, __ATOMIC_RELAXED, __HIP_MEMORY_SCOPE_AGENT);
    if (__all(f == tag)) break;
    __builtin_amdgcn_s_sleep(1);
  }
  asm volatile("" ::: "memory");
  u32x4 pv[3][4]; u32x2 pm[3];
#pragma unroll
  for (int k = 0; k < 3; ++k) {
    const int src = (split + 1 + k) & 3;
    pm[k] = __builtin_amdgcn_raw_buffer_load_b64(rml, (wave * 32 + c) * 8, src * (ROWS * 8), 16);
#pragma unroll
    for (int j = 0; j < 4; ++j) pv[k][j] = __builtin_amdgcn_raw_buffer_load_b128(rpo, pvo + j * 1024, (src * 4 + split) * 16384, 16);
  }
  if (tid == 0 && split == 0) __hip_atomic_store(ep, tag, __ATOMIC_RELAXED, __HIP_MEMORY_SCOPE_AGENT);
  const float m1 = __uint_as_float(pm[0][0]), m2 = __uint_as_float(pm[1][0]), m3 = __uint_as_float(pm[2][0]);
  const float M = fmaxf(fmaxf(mL, m1), fmaxf(m2, m3));
  const float w0 = __builtin_amdgcn_exp2f(mL - M), w1 = __builtin_amdgcn_exp2f(m1 - M), w2 = __builtin_amdgcn_exp2f(m2 - M), w3 = __builtin_amdgcn_exp2f(m3 - M);
  const float inv = 1.f / (w0 * l + w1 * __uint_as_float(pm[0][1]) + w2 * __uint_as_float(pm[1][1]) + w3 * __uint_as_float(pm[2][1]));
  uint16_t* orow = out + ((size_t)pair * ROWS + wave * 32 + c) * DH + 32 * split + 4 * hh;
#pragma unroll
  for (int j = 0; j < 4; ++j) {
    float r[4];
#pragma unroll
    for (int e = 0; e < 4; ++e)
      r[e] = (w0 * own[4 * j + e] + w1 * __uint_as_float(pv[0][j][e]) + w2 * __uint_as_float(pv[1][j][e]) + w3 * __uint_as_float(pv[2][j][e])) * inv;
    *(u32x2*)(orow + 8 * j) = u32x2{cvtpk(r[0], r[1]), cvtpk(r[2], r[3])};
  }
}

extern "C" void kernel_launch(void* const* d_in, const int* in_sizes, int n_in, void* d_out, int out_size, void* d_ws, size_t ws_size,
                              hipStream_t stream) {
  hipLaunchKernelGGL(attn_split, dim3(NPAIR * NSPLIT), dim3(512), 0, stream, (const uint16_t*)d_in[0], (const int*)d_in[1], (const int*)d_in[2],
                     (const uint16_t*)d_in[3], (const uint16_t*)d_in[4], (const uint16_t*)d_in[5], (const uint16_t*)d_in[6], (char*)d_ws, (uint16_t*)d_out);
}
```

```cpp
#include <hip/hip_runtime.h>
#include <hip/hip_bf16.h>
#include <stdint.h>
#include <type_traits>

#define KV_AUX 2
#define SC_AUX 2
#define Q_NT 0
#define RESCALE_THR 6.0f
#define PROD_PRIO 2
#define XMAP 2
#define NSTATIC 20

constexpr int B_ = 16, HQ = 32, HKV = 4, SQ = 16, SKV = 4096, DH = 128, GRP = 32;
constexpr int REP = HQ / HKV;
constexpr int ROWS = REP * SQ;
constexpr int NPAIR = B_ * HKV;
constexpr int NSPLIT = 4;
constexpr int TILE = 32;
constexpr int NTILE = SKV / TILE;
static_assert(TILE == GRP, "tile == K quant group");
static_assert(NSTATIC >= 6 && NSTATIC <= 32, "static tiles");

constexpr int KBLK = 272;
constexpr int KT_BYTES = 32 * KBLK;
constexpr int VROW = 320;
constexpr int VT_BYTES = TILE * VROW;
constexpr int SLOT = KT_BYTES + VT_BYTES;
constexpr int TABW = 1088;
constexpr int L_TAB = 2 * SLOT;
constexpr int L_RING = L_TAB + 4 * TABW;
constexpr int LDS_BYTES = L_RING + 64;

constexpr size_t WS_E = 0;
constexpr size_t WS_F = 16384;
constexpr size_t WS_PML = 65536;
constexpr size_t WS_PO = WS_PML + (size_t)NPAIR * NSPLIT * ROWS * 8;
constexpr int PO_BLK = 4 * 2048;
constexpr int PO_PAIR = NSPLIT * 4 * PO_BLK;

typedef short bf16x8 __attribute__((ext_vector_type(8)));
typedef short s16x4 __attribute__((ext_vector_type(4)));
typedef float f32x16 __attribute__((ext_vector_type(16)));
typedef float f32x2 __attribute__((ext_vector_type(2)));
typedef unsigned u32x4 __attribute__((ext_vector_type(4)));
typedef unsigned u32x2 __attribute__((ext_vector_type(2)));
typedef __bf16 bf16x2_t __attribute__((ext_vector_type(2)));
typedef __attribute__((address_space(3))) s16x4* lds_s16x4p;
typedef __attribute__((address_space(3))) char lds_char;
typedef __attribute__((address_space(1))) unsigned long long gu64;
typedef __attribute__((address_space(1))) unsigned gu32;

__device__ __forceinline__ unsigned cvtpk(float lo, float hi) {
  f32x2 v = {lo, hi};
  bf16x2_t r = __builtin_convertvector(v, bf16x2_t);
  return __builtin_bit_cast(unsigned, r);
}
__device__ __forceinline__ float bflo(unsigned u) { return __uint_as_float(u << 16); }
__device__ __forceinline__ float bfhi(unsigned u) { return __uint_as_float(u & 0xffff0000u); }
__device__ __forceinline__ unsigned pku16(unsigned a, unsigned b) {
  return __builtin_bit_cast(unsigned, __builtin_amdgcn_cvt_pk_u16(a, b));
}
__device__ __forceinline__ unsigned deq2(float qa, float za, float sa, float qb, float zb, float sb) {
  const unsigned a = cvtpk(qa - za, qb - zb);
  return cvtpk(bflo(a) * sa, bfhi(a) * sb);
}
__device__ __forceinline__ unsigned lut2(unsigned t, unsigned tlo, unsigned thi) {
  return __builtin_amdgcn_perm(thi, tlo, __umul24(t, 0x0202u) + 0x01000100u);
}
__device__ __forceinline__ bf16x8 trread2(lds_char* p0, lds_char* p1) {
  const s16x4 a = __builtin_amdgcn_ds_read_tr16_b64_v4i16((lds_s16x4p)p0);
  const s16x4 b = __builtin_amdgcn_ds_read_tr16_b64_v4i16((lds_s16x4p)p1);
  return __builtin_shufflevector(a, b, 0, 1, 2, 3, 4, 5, 6, 7);
}

constexpr float SCALE = 0.08838834764831845f;
constexpr float L2E = 1.4426950408889634f;

__device__ __forceinline__ void wg_barrier() {
  asm volatile("s_waitcnt lgkmcnt(0)" ::: "memory");
  __builtin_amdgcn_s_barrier();
  asm volatile("" ::: "memory");
  __builtin_amdgcn_sched_barrier(0);
}
#define PIN8(R) asm volatile("" : "+v"(R[0]), "+v"(R[1]), "+v"(R[2]), "+v"(R[3]), "+v"(R[4]), "+v"(R[5]), "+v"(R[6]), "+v"(R[7]))

__global__ __launch_bounds__(512, 2) void attn_split(const uint16_t* __restrict__ q, const int* __restrict__ qk, const int* __restrict__ qv,
                                                     const uint16_t* __restrict__ ksc, const uint16_t* __restrict__ kzr,
                                                     const uint16_t* __restrict__ vsc, const uint16_t* __restrict__ vzr,
                                                     char* __restrict__ ws, uint16_t* __restrict__ out) {
  __shared__ __attribute__((aligned(16))) char lds[LDS_BYTES];
  const int tid = threadIdx.x, lane = tid & 63;
  const int wave = __builtin_amdgcn_readfirstlane(tid >> 6);
  const int bid = blockIdx.x;
  int pair, split;
  if (XMAP == 0) { pair = (bid & 7) + 8 * (bid >> 5); split = (bid >> 3) & 3; }
  else if (XMAP == 1) { pair = bid >> 2; split = bid & 3; }
  else {
    const int g = bid >> 5; split = (bid >> 3) & 3;
    const int a = (0x76532153 >> (4 * g)) & 7;
    pair = 8 * g + ((bid - split * a) & 7);
  }
  const int c = lane & 31, hh = lane >> 5;
  volatile __attribute__((address_space(3))) int* ring = (volatile __attribute__((address_space(3))) int*)((lds_char*)lds + L_RING);
  gu64* ep = (gu64*)(ws + WS_E + (size_t)pair * 256);

  if (wave >= 4) {
    if (PROD_PRIO) __builtin_amdgcn_s_setprio(PROD_PRIO);
    const int pw = wave - 4, isV = pw >> 1, u = pw & 1;
    const int* base = (isV ? qv : qk) + (size_t)pair * SKV * DH;
    const __amdgpu_buffer_rsrc_t rs = __builtin_amdgcn_make_buffer_rsrc((void*)base, 0, SKV * DH * 4, 0x00020000);
    const uint16_t* sbase = isV ? vsc + (size_t)pair * SKV : ksc + (size_t)pair * NTILE * DH;
    const uint16_t* zbase = isV ? vzr + (size_t)pair * SKV : kzr + (size_t)pair * NTILE * DH;
    const int sbytes = isV ? SKV * 2 : NTILE * DH * 2, sstride = isV ? TILE * 2 : DH * 2;
    const __amdgpu_buffer_rsrc_t rss = __builtin_amdgcn_make_buffer_rsrc((void*)sbase, 0, sbytes, 0x00020000);
    const __amdgpu_buffer_rsrc_t rsz = __builtin_amdgcn_make_buffer_rsrc((void*)zbase, 0, sbytes, 0x00020000);
    const int krow = 16 * u + 8 * hh;
    const int voff = krow * 512 + c * 16;
    const int soff = isV ? 32 * u + 4 * (lane & 7) : 4 * lane;
    char* const tabw = lds + L_TAB + pw * TABW + lane * 16;
    const char* const tabr = lds + L_TAB + pw * TABW + (isV ? hh * 64 : c * 32);
    char* const wr = isV ? lds + KT_BYTES + krow * VROW + c * 8 : lds + c * KBLK + (2 * u + hh) * 16;
    u32x4 raw[2][8]; unsigned sc[2], zr[2], ticket[2] = {0u, 0u};
    int tid_[2] = {split, 4 + split};
    const __amdgpu_buffer_rsrc_t rtk = __builtin_amdgcn_make_buffer_rsrc((void*)(ws + WS_E + (size_t)pair * 256), 0, 256, 0x00020000);
    constexpr int TK_OFF = 32 << 20;
    const int tkoff = tid == 256 ? 128 : TK_OFF;
#define LOADT(J, T) do { int so_ = soff + (T) * sstride, vo_ = voff + (T) * 16384; asm volatile("" : "+v"(vo_), "+v"(so_)); \
      sc[J] = __builtin_amdgcn_raw_buffer_load_b32(rss, so_, 0, SC_AUX); zr[J] = __builtin_amdgcn_raw_buffer_load_b32(rsz, so_, 0, SC_AUX); \
      _Pragma("unroll") for (int i = 0; i < 8; ++i) raw[J][i] = __builtin_amdgcn_raw_buffer_load_b128(rs, vo_ + i * 512, 0, KV_AUX); } while (0)
    LOADT(0, tid_[0]);
    ticket[1] = (unsigned)__builtin_amdgcn_raw_ptr_buffer_atomic_add_i32(1, rtk, TK_OFF + 4 * lane, 0, 0);
    LOADT(1, tid_[1]);
    auto step = [&](auto jc, int n) {
      constexpr int J = decltype(jc)::value;
      char* dst = wr + J * SLOT;
      const int tcur = tid_[J];
      asm volatile("" : "+v"(sc[J]), "+v"(zr[J]));
      {
        const float sl = bflo(sc[J]), sh = bfhi(sc[J]), zl = bflo(zr[J]), zh = bfhi(zr[J]);
        const u32x4 e = {deq2(0.f, zl, sl, 1.f, zl, sl), deq2(2.f, zl, sl, 3.f, zl, sl), deq2(0.f, zh, sh, 1.f, zh, sh), deq2(2.f, zh, sh, 3.f, zh, sh)};
        *(u32x4*)tabw = e;
      }
      u32x4 tv[4];
#pragma unroll
      for (int w = 0; w < 4; ++w) tv[w] = *(const u32x4*)(tabr + w * 16);
      __builtin_amdgcn_sched_barrier(0);
      PIN8(raw[J]);
      if (tid == 256 && n + 2 >= NSTATIC) ring[(n + 2) & 7] = 4 * NSTATIC + (int)(ticket[J] & 0xffffu);
      unsigned tp[16], o[16], bad = 0;
      if (!isV) {
#pragma unroll
        for (int k = 0; k < 16; ++k) { tp[k] = pku16(raw[J][2 * (k & 3)][k >> 2], raw[J][2 * (k & 3) + 1][k >> 2]); bad |= tp[k]; }
        if (__builtin_expect(__any((bad & 0xfffcfffcu) != 0), 0)) {
          const u32x2 s2 = *(const u32x2*)(sbase + tcur * DH + 4 * c), z2 = *(const u32x2*)(zbase + tcur * DH + 4 * c);
#pragma unroll
          for (int k = 0; k < 16; ++k) {
            const int e = k >> 2;
            const float sf = (e & 1) ? bfhi(s2[e >> 1]) : bflo(s2[e >> 1]), zf = (e & 1) ? bfhi(z2[e >> 1]) : bflo(z2[e >> 1]);
            const unsigned qq = cvtpk((float)(int)raw[J][2 * (k & 3)][e], (float)(int)raw[J][2 * (k & 3) + 1][e]);
            o[k] = deq2(bflo(qq), zf, sf, bfhi(qq), zf, sf);
          }
        } else {
#pragma unroll
          for (int k = 0; k < 16; ++k) o[k] = lut2(tp[k], tv[k >> 3][2 * ((k >> 2) & 1)], tv[k >> 3][2 * ((k >> 2) & 1) + 1]);
        }
#pragma unroll
        for (int e = 0; e < 4; ++e) *(u32x4*)(dst + e * 64) = u32x4{o[4 * e], o[4 * e + 1], o[4 * e + 2], o[4 * e + 3]};
      } else {
#pragma unroll
        for (int k = 0; k < 16; ++k) { tp[k] = pku16(raw[J][k >> 1][2 * (k & 1)], raw[J][k >> 1][2 * (k & 1) + 1]); bad |= tp[k]; }
        if (__builtin_expect(__any((bad & 0xfffcfffcu) != 0), 0)) {
          const u32x4 s8 = *(const u32x4*)(sbase + tcur * TILE + krow), z8 = *(const u32x4*)(zbase + tcur * TILE + krow);
#pragma unroll
          for (int k = 0; k < 16; ++k) {
            const int i = k >> 1;
            const float sf = (i & 1) ? bfhi(s8[i >> 1]) : bflo(s8[i >> 1]), zf = (i & 1) ? bfhi(z8[i >> 1]) : bflo(z8[i >> 1]);
            const unsigned qq = cvtpk((float)(int)raw[J][i][2 * (k & 1)], (float)(int)raw[J][i][2 * (k & 1) + 1]);
            o[k] = deq2(bflo(qq), zf, sf, bfhi(qq), zf, sf);
          }
        } else {
#pragma unroll
          for (int k = 0; k < 16; ++k) o[k] = lut2(tp[k], tv[k >> 2][2 * ((k >> 1) & 1)], tv[k >> 2][2 * ((k >> 1) & 1) + 1]);
        }
#pragma unroll
        for (int i = 0; i < 8; ++i) *(u32x2*)(dst + i * VROW) = u32x2{o[2 * i], o[2 * i + 1]};
      }
      int tn = NTILE;
      if (J == 0 || tcur < NTILE) {
        wg_barrier();
        tn = n + 2 < NSTATIC ? 4 * (n + 2) + split : __builtin_amdgcn_readfirstlane(ring[(n + 2) & 7]);
      }
      __builtin_amdgcn_sched_barrier(0);
      ticket[J] = (unsigned)__builtin_amdgcn_raw_ptr_buffer_atomic_add_i32(1, rtk, n + 4 >= NSTATIC ? tkoff : TK_OFF, 0, 0);
      LOADT(J, tn);
      tid_[J] = tn;
      __builtin_amdgcn_sched_barrier(0);
    };
    int n = 0;
    do {
      step(std::integral_constant<int, 0>{}, n);
      step(std::integral_constant<int, 1>{}, n + 1);
      n += 2;
    } while (tid_[0] < NTILE);
#undef LOADT
    __builtin_amdgcn_s_barrier();
    return;
  }

  bf16x8 qf[8];
  {
    const uint16_t* qrow = q + ((size_t)pair * ROWS + wave * 32 + c) * DH + hh * 8;
#pragma unroll
    for (int ks = 0; ks < 8; ++ks) qf[ks] = Q_NT ? __builtin_nontemporal_load((const bf16x8*)(qrow + ks * 16)) : *(const bf16x8*)(qrow + ks * 16);
  }
  const unsigned long long tag = __hip_atomic_load(ep, __ATOMIC_RELAXED, __HIP_MEMORY_SCOPE_AGENT) + 1;
  unsigned long long tw = __hip_atomic_load(ep + 16, __ATOMIC_RELAXED, __HIP_MEMORY_SCOPE_AGENT);
  f32x16 acc0 = {}, acc1 = {}, acc2 = {}, acc3 = {};
  float m = -2.0e38f, l = 0.f;
  const int rq = (lane & 15) >> 2, rp = lane & 3, rb = (lane >> 4) & 1;
  lds_char* kr = (lds_char*)lds + 2 * hh * KBLK + rq * 64 + rb * 32 + rp * 8;
  lds_char* vr = (lds_char*)lds + KT_BYTES + (4 * hh + rq) * VROW + rb * 32 + rp * 8;
  auto cstep = [&](auto jc, int n) -> bool {
    constexpr int j = decltype(jc)::value;
    wg_barrier();
    const int tn = ring[(n + 1) & 7];
    bf16x8 kf[8], vf[8];
#pragma unroll
    for (int ks = 0; ks < 8; ++ks) kf[ks] = trread2(kr + j * SLOT + (4 * ks) * KBLK, kr + j * SLOT + (4 * ks + 1) * KBLK);
    __builtin_amdgcn_sched_barrier(0);
    f32x16 s = {};
#pragma unroll
    for (int ks = 0; ks < 8; ++ks) {
      s = __builtin_amdgcn_mfma_f32_32x32x16_bf16(kf[ks], qf[ks], s, 0, 0, 0);
      const int s2 = ks >> 2, db = ks & 3;
      vf[ks] = trread2(vr + j * SLOT + (16 * s2) * VROW + db * 64, vr + j * SLOT + (16 * s2 + 8) * VROW + db * 64);
      __builtin_amdgcn_sched_barrier(0);
    }
    float x[16];
#pragma unroll
    for (int r = 0; r < 16; r += 2) {
      const unsigned a = cvtpk(s[r], s[r + 1]);
      const unsigned b = cvtpk(bflo(a) * SCALE, bfhi(a) * SCALE);
      x[r] = bflo(b); x[r + 1] = bfhi(b);
    }
    float mx = x[0];
#pragma unroll
    for (int r = 1; r < 16; ++r) mx = fmaxf(mx, x[r]);
    {
      auto rr = __builtin_amdgcn_permlane32_swap(__float_as_uint(mx), __float_as_uint(mx), false, false);
      mx = fmaxf(__uint_as_float(rr[0]), __uint_as_float(rr[1]));
    }
    if (__any(!(mx - m <= RESCALE_THR))) {
      const float mn = fmaxf(m, mx);
      const float alpha = __builtin_amdgcn_exp2f((m - mn) * L2E);
      m = mn;
      l *= alpha;
      acc0 *= alpha; acc1 *= alpha; acc2 *= alpha; acc3 *= alpha;
    }
    const float nm = -m * L2E;
    float ps = 0.f;
#pragma unroll
    for (int r = 0; r < 16; ++r) { x[r] = __builtin_amdgcn_exp2f(fmaf(x[r], L2E, nm)); ps += x[r]; }
    l += ps;
    u32x4 p0 = {cvtpk(x[0], x[1]), cvtpk(x[2], x[3]), cvtpk(x[4], x[5]), cvtpk(x[6], x[7])};
    u32x4 p1 = {cvtpk(x[8], x[9]), cvtpk(x[10], x[11]), cvtpk(x[12], x[13]), cvtpk(x[14], x[15])};
    const bf16x8 pf0 = __builtin_bit_cast(bf16x8, p0), pf1 = __builtin_bit_cast(bf16x8, p1);
    acc0 = __builtin_amdgcn_mfma_f32_32x32x16_bf16(vf[0], pf0, acc0, 0, 0, 0);
    acc1 = __builtin_amdgcn_mfma_f32_32x32x16_bf16(vf[1], pf0, acc1, 0, 0, 0);
    acc2 = __builtin_amdgcn_mfma_f32_32x32x16_bf16(vf[2], pf0, acc2, 0, 0, 0);
    acc3 = __builtin_amdgcn_mfma_f32_32x32x16_bf16(vf[3], pf0, acc3, 0, 0, 0);
    acc0 = __builtin_amdgcn_mfma_f32_32x32x16_bf16(vf[4], pf1, acc0, 0, 0, 0);
    acc1 = __builtin_amdgcn_mfma_f32_32x32x16_bf16(vf[5], pf1, acc1, 0, 0, 0);
    acc2 = __builtin_amdgcn_mfma_f32_32x32x16_bf16(vf[6], pf1, acc2, 0, 0, 0);
    acc3 = __builtin_amdgcn_mfma_f32_32x32x16_bf16(vf[7], pf1, acc3, 0, 0, 0);
    if (n <= 1 && tid == 0) {
      if (n == 0) {
        if ((tw >> 16) != (tag & 0xffffffffffffull))
          __hip_atomic_compare_exchange_strong(ep + 16, &tw, tag << 16, __ATOMIC_RELAXED, __ATOMIC_RELAXED, __HIP_MEMORY_SCOPE_AGENT);
      } else {
        asm volatile("" :: "v"(tw));
      }
    }
    return n + 1 < NSTATIC || __builtin_amdgcn_readfirstlane(tn) < NTILE;
  };
  for (int n = 0;; n += 2) {
    if (!cstep(std::integral_constant<int, 0>{}, n)) break;
    if (!cstep(std::integral_constant<int, 1>{}, n + 1)) break;
  }
  {
    auto rr = __builtin_amdgcn_permlane32_swap(__float_as_uint(l), __float_as_uint(l), false, false);
    l = __uint_as_float(rr[0]) + __uint_as_float(rr[1]);
  }
  const float mL = m * L2E;
  const __amdgpu_buffer_rsrc_t rpo = __builtin_amdgcn_make_buffer_rsrc((void*)(ws + WS_PO + (size_t)pair * PO_PAIR), 0, PO_PAIR, 0x00020000);
  const __amdgpu_buffer_rsrc_t rml = __builtin_amdgcn_make_buffer_rsrc((void*)(ws + WS_PML + (size_t)pair * (NSPLIT * ROWS * 8)), 0, NSPLIT * ROWS * 8, 0x00020000);
  const int pvo = wave * 2048 + lane * 16;
  auto publish = [&](const f32x16& a, int db) {
    if (db == split) return;
#pragma unroll
    for (int j = 0; j < 2; ++j) {
      const u32x4 v = {cvtpk(a[8 * j], a[8 * j + 1]), cvtpk(a[8 * j + 2], a[8 * j + 3]), cvtpk(a[8 * j + 4], a[8 * j + 5]), cvtpk(a[8 * j + 6], a[8 * j + 7])};
      __builtin_amdgcn_raw_buffer_store_b128(v, rpo, pvo + j * 1024, (split * 4 + db) * PO_BLK, 16);
    }
  };
  publish(acc0, 0); publish(acc1, 1); publish(acc2, 2); publish(acc3, 3);
  if (hh == 0) {
    const u32x2 ml = {__float_as_uint(mL), __float_as_uint(l)};
    __builtin_amdgcn_raw_buffer_store_b64(ml, rml, (wave * 32 + c) * 8, split * (ROWS * 8), 16);
  }
  gu64* fl = (gu64*)(ws + WS_F + (size_t)pair * 512);
  asm volatile("s_waitcnt vmcnt(0)" ::: "memory");
  __builtin_amdgcn_s_barrier();
  asm volatile("" ::: "memory");
  if (tid == 0) __hip_atomic_store(fl + split * 16, tag, __ATOMIC_RELAXED, __HIP_MEMORY_SCOPE_AGENT);
  f32x16 own;
  if (split == 0) own = acc0; else if (split == 1) own = acc1; else if (split == 2) own = acc2; else own = acc3;
  for (unsigned spins = 0; spins < (1u << 20); ++spins) {
    unsigned long long f = tag;
    if (lane < 4 && lane != split) f = __hip_atomic_load(fl + lane * 16, __ATOMIC_RELAXED, __HIP_MEMORY_SCOPE_AGENT);
    if (__all(f == tag)) break;
    __builtin_amdgcn_s_sleep(1);
  }
  asm volatile("" ::: "memory");
  u32x4 pv[3][2]; u32x2 pm[3];
#pragma unroll
  for (int k = 0; k < 3; ++k) {
    const int src = (split + 1 + k) & 3;
    pm[k] = __builtin_amdgcn_raw_buffer_load_b64(rml, (wave * 32 + c) * 8, src * (ROWS * 8), 16);
#pragma unroll
    for (int j = 0; j < 2; ++j) pv[k][j] = __builtin_amdgcn_raw_buffer_load_b128(rpo, pvo + j * 1024, (src * 4 + split) * PO_BLK, 16);
  }
  if (tid == 0 && split == 0) __hip_atomic_store(ep, tag, __ATOMIC_RELAXED, __HIP_MEMORY_SCOPE_AGENT);
  const float m1 = __uint_as_float(pm[0][0]), m2 = __uint_as_float(pm[1][0]), m3 = __uint_as_float(pm[2][0]);
  const float M = fmaxf(fmaxf(mL, m1), fmaxf(m2, m3));
  const float w0 = __builtin_amdgcn_exp2f(mL - M), w1 = __builtin_amdgcn_exp2f(m1 - M), w2 = __builtin_amdgcn_exp2f(m2 - M), w3 = __builtin_amdgcn_exp2f(m3 - M);
  const float inv = 1.f / (w0 * l + w1 * __uint_as_float(pm[0][1]) + w2 * __uint_as_float(pm[1][1]) + w3 * __uint_as_float(pm[2][1]));
  uint16_t* orow = out + ((size_t)pair * ROWS + wave * 32 + c) * DH + 32 * split + 4 * hh;
#pragma unroll
  for (int j = 0; j < 4; ++j) {
    float r[4];
#pragma unroll
    for (int e = 0; e < 4; ++e) {
      const int w = (4 * j + e) >> 1;
      const unsigned a1 = pv[0][w >> 2][w & 3], a2 = pv[1][w >> 2][w & 3], a3 = pv[2][w >> 2][w & 3];
      const float f1 = (e & 1) ? bfhi(a1) : bflo(a1), f2 = (e & 1) ? bfhi(a2) : bflo(a2), f3 = (e & 1) ? bfhi(a3) : bflo(a3);
      r[e] = (w0 * own[4 * j + e] + w1 * f1 + w2 * f2 + w3 * f3) * inv;
    }
    *(u32x2*)(orow + 8 * j) = u32x2{cvtpk(r[0], r[1]), cvtpk(r[2], r[3])};
  }
}

extern "C" void kernel_launch(void* const* d_in, const int* in_sizes, int n_in, void* d_out, int out_size, void* d_ws, size_t ws_size,
                              hipStream_t stream) {
  hipLaunchKernelGGL(attn_split, dim3(NPAIR * NSPLIT), dim3(512), 0, stream, (const uint16_t*)d_in[0], (const int*)d_in[1], (const int*)d_in[2],
                     (const uint16_t*)d_in[3], (const uint16_t*)d_in[4], (const uint16_t*)d_in[5], (const uint16_t*)d_in[6], (char*)d_ws, (uint16_t*)d_out);
}
```

```cpp
#include <hip/hip_runtime.h>
#include <hip/hip_bf16.h>
#include <stdint.h>
#include <type_traits>

#define RESCALE_THR 6.0f
#define PROD_PRIO 2
#define PF_CODE 16
#define PF_TLB 0
#define OUT_AUX 0

constexpr int B_ = 16, HQ = 32, HKV = 4, SQ = 16, SKV = 4096, DH = 128, GRP = 32;
constexpr int REP = HQ / HKV;
constexpr int ROWS = REP * SQ;
constexpr int NPAIR = B_ * HKV;
constexpr int NSPLIT = 4;
constexpr int TILE = 32;
constexpr int NTILE = SKV / TILE;
constexpr int NT = NTILE / NSPLIT;
constexpr int RING = 2;
constexpr int NTAUX = 2;
static_assert(TILE == GRP, "tile == K quant group");
static_assert(NT % RING == 0, "ring");

constexpr int KBLK = 272;
constexpr int KT_BYTES = 32 * KBLK;
constexpr int VROW = 320;
constexpr int VT_BYTES = TILE * VROW;
constexpr int SLOT = KT_BYTES + VT_BYTES;
constexpr int TABW = 1024;
constexpr int L_TAB = 2 * SLOT;
constexpr int QROW = 272;
constexpr int L_Q = L_TAB + 4 * TABW;
constexpr int LDS_BYTES = L_Q + ROWS * QROW;

constexpr size_t WS_E = 0;
constexpr size_t WS_F = 32768;
constexpr size_t WS_PML = 262144;
constexpr size_t WS_PO = WS_PML + (size_t)NPAIR * NSPLIT * ROWS * 8;
constexpr int PO_BLK = 4 * 2048;
constexpr int PO_PAIR = NSPLIT * 4 * PO_BLK;

typedef short bf16x8 __attribute__((ext_vector_type(8)));
typedef short s16x4 __attribute__((ext_vector_type(4)));
typedef float f32x16 __attribute__((ext_vector_type(16)));
typedef float f32x2 __attribute__((ext_vector_type(2)));
typedef unsigned u32x4 __attribute__((ext_vector_type(4)));
typedef unsigned u32x2 __attribute__((ext_vector_type(2)));
typedef __bf16 bf16x2_t __attribute__((ext_vector_type(2)));
typedef __attribute__((address_space(3))) s16x4* lds_s16x4p;
typedef __attribute__((address_space(3))) char lds_char;
typedef __attribute__((address_space(1))) unsigned long long gu64;

__device__ __forceinline__ unsigned cvtpk(float lo, float hi) {
  f32x2 v = {lo, hi};
  bf16x2_t r = __builtin_convertvector(v, bf16x2_t);
  return __builtin_bit_cast(unsigned, r);
}
__device__ __forceinline__ float bflo(unsigned u) { return __uint_as_float(u << 16); }
__device__ __forceinline__ float bfhi(unsigned u) { return __uint_as_float(u & 0xffff0000u); }
__device__ __forceinline__ unsigned pku16(unsigned a, unsigned b) {
  return __builtin_bit_cast(unsigned, __builtin_amdgcn_cvt_pk_u16(a, b));
}
__device__ __forceinline__ unsigned deq2(float qa, float za, float sa, float qb, float zb, float sb) {
  const unsigned a = cvtpk(qa - za, qb - zb);
  return cvtpk(bflo(a) * sa, bfhi(a) * sb);
}
__device__ __forceinline__ unsigned lut2(unsigned t, unsigned tlo, unsigned thi) {
  return __builtin_amdgcn_perm(thi, tlo, __umul24(t, 0x0202u) + 0x01000100u);
}
__device__ __forceinline__ bf16x8 trread2(lds_char* p0, lds_char* p1) {
  const s16x4 a = __builtin_amdgcn_ds_read_tr16_b64_v4i16((lds_s16x4p)p0);
  const s16x4 b = __builtin_amdgcn_ds_read_tr16_b64_v4i16((lds_s16x4p)p1);
  return __builtin_shufflevector(a, b, 0, 1, 2, 3, 4, 5, 6, 7);
}
__device__ __forceinline__ __amdgpu_buffer_rsrc_t rsrc(const void* p, int bytes) {
  return __builtin_amdgcn_make_buffer_rsrc((void*)p, 0, bytes, 0x00020000);
}

constexpr float SCALE = 0.08838834764831845f;
constexpr float L2E = 1.4426950408889634f;

__device__ __forceinline__ void wg_barrier() {
  asm volatile("s_waitcnt lgkmcnt(0)" ::: "memory");
  __builtin_amdgcn_s_barrier();
  asm volatile("" ::: "memory");
  __builtin_amdgcn_sched_barrier(0);
}

__global__ __launch_bounds__(512, 2) void attn_split(const uint16_t* __restrict__ q_, const int* __restrict__ qk_, const int* __restrict__ qv_,
                                                     const uint16_t* __restrict__ ksc_, const uint16_t* __restrict__ kzr_,
                                                     const uint16_t* __restrict__ vsc_, const uint16_t* __restrict__ vzr_,
                                                     char* __restrict__ ws, uint16_t* __restrict__ out) {
  __shared__ __attribute__((aligned(16))) char lds[LDS_BYTES];
  const int tid = threadIdx.x, lane = tid & 63;
  const int wave = __builtin_amdgcn_readfirstlane(tid >> 6);
  const int bid = blockIdx.x;
  const int pair = (bid & 7) + 8 * (bid >> 5), split = (bid >> 3) & 3;
  const int c = lane & 31, hh = lane >> 5;
  unsigned pfc = 0;
  if (PF_CODE) {
    const uintptr_t pc = (uintptr_t)__builtin_amdgcn_s_getpc() & ~(uintptr_t)127;
    if (tid < PF_CODE * 8) pfc = *(const __attribute__((address_space(1))) unsigned*)(pc + tid * 128);
  }

  if (wave < 4) {
    if (PROD_PRIO) __builtin_amdgcn_s_setprio(PROD_PRIO);
    const int u = wave & 1;
    auto produce = [&](auto vc) {
      constexpr bool isV = decltype(vc)::value;
      const char *q = (const char*)q_, *kv = (const char*)(isV ? qv_ : qk_), *sp = (const char*)(isV ? vsc_ : ksc_), *zp = (const char*)(isV ? vzr_ : kzr_);
      constexpr int sbytes = isV ? SKV * 2 : NTILE * DH * 2, sstride = isV ? TILE * 2 : DH * 2;
      const __amdgpu_buffer_rsrc_t rs = rsrc(kv + (size_t)pair * (SKV * DH * 4), SKV * DH * 4);
      const __amdgpu_buffer_rsrc_t rss = rsrc(sp + (size_t)pair * sbytes, sbytes), rsz = rsrc(zp + (size_t)pair * sbytes, sbytes);
      const int krow = 16 * u + 8 * hh;
      const int voff = krow * 512 + c * 16;
      const int soff = isV ? 32 * u + 4 * (lane & 7) : 4 * lane;
      char* const tabw = lds + L_TAB + wave * TABW + lane * 16;
      const char* const tabr = lds + L_TAB + wave * TABW + (isV ? hh * 64 : c * 32);
      char* const wr = isV ? lds + KT_BYTES + krow * VROW + c * 8 : lds + c * KBLK + (2 * u + hh) * 16;
      u32x4 raw[RING][8]; unsigned sc[RING], zr[RING];
#define LOADS(J, T) do { int so_ = soff + (T) * sstride; asm volatile("" : "+v"(so_)); \
        sc[J] = __builtin_amdgcn_raw_buffer_load_b32(rss, so_, 0, NTAUX); zr[J] = __builtin_amdgcn_raw_buffer_load_b32(rsz, so_, 0, NTAUX); } while (0)
#define LOADT(J, T) do { LOADS(J, T); int vo_ = voff + (T) * 16384; asm volatile("" : "+v"(vo_)); \
        _Pragma("unroll") for (int i = 0; i < 8; ++i) raw[J][i] = __builtin_amdgcn_raw_buffer_load_b128(rs, vo_ + i * 512, 0, NTAUX); } while (0)
      if constexpr (isV) {
        const __amdgpu_buffer_rsrc_t rq = rsrc(q + (size_t)pair * (ROWS * DH * 2), ROWS * DH * 2);
        u32x4 qt[16];
#pragma unroll
        for (int i = 0; i < 16; ++i) qt[i] = __builtin_amdgcn_raw_buffer_load_b128(rq, u * 16384 + lane * 16 + (i & 3) * 1024, (i >> 2) * 4096, 0);
        __builtin_amdgcn_sched_barrier(0);
        LOADT(0, split);
        __builtin_amdgcn_sched_barrier(0);
        char* qs = lds + L_Q + (u * 64 + (lane >> 4)) * QROW + (lane & 15) * 16;
#pragma unroll
        for (int i = 0; i < 16; ++i) *(u32x4*)(qs + 4 * i * QROW) = qt[i];
        wg_barrier();
        LOADT(1, 4 + split);
      } else {
        LOADT(0, split);
        LOADT(1, 4 + split);
      }
      auto step = [&](auto jc, int n) {
        constexpr int J = decltype(jc)::value;
        char* dst = wr + J * SLOT;
        const int tcur = 4 * n + split;
        asm volatile("" : "+v"(sc[J]), "+v"(zr[J]));
        {
          const float sl = bflo(sc[J]), sh = bfhi(sc[J]), zl = bflo(zr[J]), zh = bfhi(zr[J]);
          const u32x4 e = {deq2(0.f, zl, sl, 1.f, zl, sl), deq2(2.f, zl, sl, 3.f, zl, sl), deq2(0.f, zh, sh, 1.f, zh, sh), deq2(2.f, zh, sh, 3.f, zh, sh)};
          *(u32x4*)tabw = e;
        }
        u32x4 tv[4];
#pragma unroll
        for (int w = 0; w < (isV ? 4 : 2); ++w) tv[w] = *(const u32x4*)(tabr + w * 16);
        __builtin_amdgcn_sched_barrier(0);
        int vn = voff + (tcur + 4 * RING) * 16384;
        LOADS(J, tcur + 4 * RING);
        asm volatile("" : "+v"(vn));
        unsigned tp[16], o[16], bad = 0;
#pragma unroll
        for (int p = 0; p < 4; ++p) {
          asm volatile("" : "+v"(raw[J][2 * p]), "+v"(raw[J][2 * p + 1]));
#pragma unroll
          for (int e = 0; e < 4; ++e) {
            const int k = isV ? 4 * p + e : 4 * e + p;
            tp[k] = isV ? pku16(raw[J][2 * p + (e >> 1)][2 * (e & 1)], raw[J][2 * p + (e >> 1)][2 * (e & 1) + 1]) : pku16(raw[J][2 * p][e], raw[J][2 * p + 1][e]);
          }
          __builtin_amdgcn_sched_barrier(0);
          raw[J][2 * p] = __builtin_amdgcn_raw_buffer_load_b128(rs, vn + 2 * p * 512, 0, NTAUX);
          raw[J][2 * p + 1] = __builtin_amdgcn_raw_buffer_load_b128(rs, vn + (2 * p + 1) * 512, 0, NTAUX);
          __builtin_amdgcn_sched_barrier(0);
        }
#pragma unroll
        for (int k = 0; k < 16; ++k) bad |= tp[k];
        if (__builtin_expect(__any((bad & 0xfffcfffcu) != 0), 0)) {
          u32x4 r[8];
#pragma unroll
          for (int i = 0; i < 8; ++i) r[i] = __builtin_amdgcn_raw_buffer_load_b128(rs, voff + tcur * 16384 + i * 512, 0, 0);
          if constexpr (!isV) {
            const u32x2 s2 = __builtin_amdgcn_raw_buffer_load_b64(rss, tcur * sstride + 8 * c, 0, 0), z2 = __builtin_amdgcn_raw_buffer_load_b64(rsz, tcur * sstride + 8 * c, 0, 0);
#pragma unroll
            for (int k = 0; k < 16; ++k) {
              const int e = k >> 2;
              const float sf = (e & 1) ? bfhi(s2[e >> 1]) : bflo(s2[e >> 1]), zf = (e & 1) ? bfhi(z2[e >> 1]) : bflo(z2[e >> 1]);
              const unsigned qq = cvtpk((float)(int)r[2 * (k & 3)][e], (float)(int)r[2 * (k & 3) + 1][e]);
              o[k] = deq2(bflo(qq), zf, sf, bfhi(qq), zf, sf);
            }
          } else {
            const u32x4 s8 = __builtin_amdgcn_raw_buffer_load_b128(rss, tcur * sstride + 2 * krow, 0, 0), z8 = __builtin_amdgcn_raw_buffer_load_b128(rsz, tcur * sstride + 2 * krow, 0, 0);
#pragma unroll
            for (int k = 0; k < 16; ++k) {
              const int i = k >> 1;
              const float sf = (i & 1) ? bfhi(s8[i >> 1]) : bflo(s8[i >> 1]), zf = (i & 1) ? bfhi(z8[i >> 1]) : bflo(z8[i >> 1]);
              const unsigned qq = cvtpk((float)(int)r[i][2 * (k & 1)], (float)(int)r[i][2 * (k & 1) + 1]);
              o[k] = deq2(bflo(qq), zf, sf, bfhi(qq), zf, sf);
            }
          }
        } else {
#pragma unroll
          for (int k = 0; k < 16; ++k)
            o[k] = isV ? lut2(tp[k], tv[k >> 2][2 * ((k >> 1) & 1)], tv[k >> 2][2 * ((k >> 1) & 1) + 1]) : lut2(tp[k], tv[k >> 3][2 * ((k >> 2) & 1)], tv[k >> 3][2 * ((k >> 2) & 1) + 1]);
        }
        if constexpr (!isV) {
#pragma unroll
          for (int e = 0; e < 4; ++e) *(u32x4*)(dst + e * 64) = u32x4{o[4 * e], o[4 * e + 1], o[4 * e + 2], o[4 * e + 3]};
        } else {
#pragma unroll
          for (int i = 0; i < 8; ++i) *(u32x2*)(dst + i * VROW) = u32x2{o[2 * i], o[2 * i + 1]};
        }
        wg_barrier();
      };
      for (int n = 0; n < NT; n += 2) {
        step(std::integral_constant<int, 0>{}, n);
        step(std::integral_constant<int, 1>{}, n + 1);
      }
      if (!isV) wg_barrier();
#undef LOADT
#undef LOADS
    };
    if (wave >> 1) produce(std::true_type{}); else produce(std::false_type{});
    asm volatile("" :: "v"(pfc));
    return;
  }

  const int cw = wave - 4;
  gu64* ep = (gu64*)(ws + WS_E + (size_t)(pair * 4 + cw) * 128);
  const unsigned long long tag = __hip_atomic_load(ep, __ATOMIC_RELAXED, __HIP_MEMORY_SCOPE_AGENT) + 1;
  if (PF_TLB) {
    constexpr int NP = (SKV * DH * 4) >> PF_TLB;
    const int w16 = split * 4 + cw;
    unsigned a = 0;
#pragma unroll
    for (int i = 0; i < (2 * NP + 1023) / 1024; ++i) {
      const int g = (i * 16 + w16) * 64 + lane;
      if (g < 2 * NP) a ^= __builtin_nontemporal_load((const unsigned*)((const char*)(g & 1 ? qv_ : qk_) + (size_t)pair * (SKV * DH * 4) + ((size_t)(g >> 1) << PF_TLB)));
    }
    asm volatile("" :: "v"(a));
  }
  bf16x8 qf[8];
  f32x16 acc0 = {}, acc1 = {}, acc2 = {}, acc3 = {};
  float m = -2.0e38f, l = 0.f;
  bf16x8 pf0 = {}, pf1 = {};
  const int rq = (lane & 15) >> 2, rp = lane & 3, rb = (lane >> 4) & 1;
  lds_char* const kr = (lds_char*)lds + 2 * hh * KBLK + rq * 64 + rb * 32 + rp * 8;
  lds_char* const vr = (lds_char*)lds + KT_BYTES + (4 * hh + rq) * VROW + rb * 32 + rp * 8;
  auto cstep = [&](auto pvc, auto qkc, int n) {
    constexpr bool PV = decltype(pvc)::value, QK = decltype(qkc)::value;
    wg_barrier();
    if constexpr (!PV) {
#pragma unroll
      for (int ks = 0; ks < 8; ++ks) qf[ks] = *(const bf16x8*)(lds + L_Q + (cw * 32 + c) * QROW + ks * 32 + hh * 16);
    }
    lds_char* kp = kr + (n & 1) * SLOT;
    lds_char* vp = vr + ((n & 1) ^ 1) * SLOT;
    bf16x8 kf[8], vf[8];
    f32x16 s = {};
    if constexpr (QK) {
#pragma unroll
      for (int ks = 0; ks < 8; ++ks) kf[ks] = trread2(kp + (4 * ks) * KBLK, kp + (4 * ks + 1) * KBLK);
      __builtin_amdgcn_sched_barrier(0);
    }
#pragma unroll
    for (int ks = 0; ks < 8; ++ks) {
      if constexpr (QK) s = __builtin_amdgcn_mfma_f32_32x32x16_bf16(kf[ks], qf[ks], s, 0, 0, 0);
      if constexpr (PV) vf[ks] = trread2(vp + (16 * (ks >> 2)) * VROW + (ks & 3) * 64, vp + (16 * (ks >> 2) + 8) * VROW + (ks & 3) * 64);
      if constexpr (QK) __builtin_amdgcn_sched_barrier(0);
    }
    if constexpr (PV) {
      acc0 = __builtin_amdgcn_mfma_f32_32x32x16_bf16(vf[0], pf0, acc0, 0, 0, 0);
      acc1 = __builtin_amdgcn_mfma_f32_32x32x16_bf16(vf[1], pf0, acc1, 0, 0, 0);
      acc2 = __builtin_amdgcn_mfma_f32_32x32x16_bf16(vf[2], pf0, acc2, 0, 0, 0);
      acc3 = __builtin_amdgcn_mfma_f32_32x32x16_bf16(vf[3], pf0, acc3, 0, 0, 0);
      acc0 = __builtin_amdgcn_mfma_f32_32x32x16_bf16(vf[4], pf1, acc0, 0, 0, 0);
      acc1 = __builtin_amdgcn_mfma_f32_32x32x16_bf16(vf[5], pf1, acc1, 0, 0, 0);
      acc2 = __builtin_amdgcn_mfma_f32_32x32x16_bf16(vf[6], pf1, acc2, 0, 0, 0);
      acc3 = __builtin_amdgcn_mfma_f32_32x32x16_bf16(vf[7], pf1, acc3, 0, 0, 0);
    }
    if constexpr (QK) {
      float x[16];
#pragma unroll
      for (int r = 0; r < 16; r += 2) {
        const unsigned a = cvtpk(s[r], s[r + 1]);
        const unsigned b = cvtpk(bflo(a) * SCALE, bfhi(a) * SCALE);
        x[r] = bflo(b); x[r + 1] = bfhi(b);
      }
      float mx = x[0];
#pragma unroll
      for (int r = 1; r < 16; ++r) mx = fmaxf(mx, x[r]);
      if constexpr (PV) {
#pragma unroll
        for (int g = 0; g < 8; ++g) {
          __builtin_amdgcn_sched_group_barrier(0x8, 1, 0);
          __builtin_amdgcn_sched_group_barrier(0x2, 8, 0);
        }
      }
      {
        auto rr = __builtin_amdgcn_permlane32_swap(__float_as_uint(mx), __float_as_uint(mx), false, false);
        mx = fmaxf(__uint_as_float(rr[0]), __uint_as_float(rr[1]));
      }
      if (__any(!(mx - m <= RESCALE_THR))) {
        const float mn = fmaxf(m, mx);
        const float alpha = __builtin_amdgcn_exp2f((m - mn) * L2E);
        m = mn;
        l *= alpha;
        acc0 *= alpha; acc1 *= alpha; acc2 *= alpha; acc3 *= alpha;
      }
      const float nm = -m * L2E;
      float ps = 0.f;
#pragma unroll
      for (int r = 0; r < 16; ++r) { x[r] = __builtin_amdgcn_exp2f(fmaf(x[r], L2E, nm)); ps += x[r]; }
      l += ps;
      const u32x4 p0 = {cvtpk(x[0], x[1]), cvtpk(x[2], x[3]), cvtpk(x[4], x[5]), cvtpk(x[6], x[7])};
      const u32x4 p1 = {cvtpk(x[8], x[9]), cvtpk(x[10], x[11]), cvtpk(x[12], x[13]), cvtpk(x[14], x[15])};
      pf0 = __builtin_bit_cast(bf16x8, p0); pf1 = __builtin_bit_cast(bf16x8, p1);
    }
  };
  cstep(std::false_type{}, std::true_type{}, 0);
#pragma nounroll
  for (int n = 1; n < NT; ++n) cstep(std::true_type{}, std::true_type{}, n);
  cstep(std::true_type{}, std::false_type{}, NT);
  {
    auto rr = __builtin_amdgcn_permlane32_swap(__float_as_uint(l), __float_as_uint(l), false, false);
    l = __uint_as_float(rr[0]) + __uint_as_float(rr[1]);
  }
  const float mL = m * L2E;
  const __amdgpu_buffer_rsrc_t rpo = rsrc(ws + WS_PO + (size_t)pair * PO_PAIR, PO_PAIR);
  const __amdgpu_buffer_rsrc_t rml = rsrc(ws + WS_PML + (size_t)pair * (NSPLIT * ROWS * 8), NSPLIT * ROWS * 8);
  const int pvo = cw * 2048 + lane * 16;
  auto publish = [&](const f32x16& a, int db) {
    if (db == split) return;
#pragma unroll
    for (int j = 0; j < 2; ++j) {
      const u32x4 v = {cvtpk(a[8 * j], a[8 * j + 1]), cvtpk(a[8 * j + 2], a[8 * j + 3]), cvtpk(a[8 * j + 4], a[8 * j + 5]), cvtpk(a[8 * j + 6], a[8 * j + 7])};
      __builtin_amdgcn_raw_buffer_store_b128(v, rpo, pvo + (split * 4 + db) * PO_BLK + j * 1024, 0, 16);
    }
  };
  publish(acc0, 0); publish(acc1, 1); publish(acc2, 2); publish(acc3, 3);
  if (hh == 0) {
    const u32x2 ml = {__float_as_uint(mL), __float_as_uint(l)};
    __builtin_amdgcn_raw_buffer_store_b64(ml, rml, (cw * 32 + c) * 8 + split * (ROWS * 8), 0, 16);
  }
  gu64* fl = (gu64*)(ws + WS_F + (size_t)(pair * 16 + cw) * 128);
  const bool poller = lane < 4 && lane != split;
  unsigned long long f = tag;
  if (poller) f = __hip_atomic_load(fl + lane * 64, __ATOMIC_RELAXED, __HIP_MEMORY_SCOPE_AGENT);
  asm volatile("s_waitcnt vmcnt(0)" ::: "memory");
  if (lane == 0) __hip_atomic_store(fl + split * 64, tag, __ATOMIC_RELAXED, __HIP_MEMORY_SCOPE_AGENT);
  f32x16 own;
  if (split == 0) own = acc0; else if (split == 1) own = acc1; else if (split == 2) own = acc2; else own = acc3;
  for (unsigned spins = 0; !__all(f == tag) && spins < (1u << 20); ++spins)
    if (poller) f = __hip_atomic_load(fl + lane * 64, __ATOMIC_RELAXED, __HIP_MEMORY_SCOPE_AGENT);
  asm volatile("" ::: "memory");
  u32x4 pv[3][2]; u32x2 pm[3];
#pragma unroll
  for (int k = 0; k < 3; ++k) {
    const int src = (split + 1 + k) & 3;
    pm[k] = __builtin_amdgcn_raw_buffer_load_b64(rml, (cw * 32 + c) * 8, src * (ROWS * 8), 16);
#pragma unroll
    for (int j = 0; j < 2; ++j) pv[k][j] = __builtin_amdgcn_raw_buffer_load_b128(rpo, pvo + j * 1024, (src * 4 + split) * PO_BLK, 16);
  }
  if (lane == 0 && split == 0) __hip_atomic_store(ep, tag, __ATOMIC_RELAXED, __HIP_MEMORY_SCOPE_AGENT);
  const float m1 = __uint_as_float(pm[0][0]), m2 = __uint_as_float(pm[1][0]), m3 = __uint_as_float(pm[2][0]);
  const float M = fmaxf(fmaxf(mL, m1), fmaxf(m2, m3));
  const float w0 = __builtin_amdgcn_exp2f(mL - M), w1 = __builtin_amdgcn_exp2f(m1 - M), w2 = __builtin_amdgcn_exp2f(m2 - M), w3 = __builtin_amdgcn_exp2f(m3 - M);
  const float inv = 1.f / (w0 * l + w1 * __uint_as_float(pm[0][1]) + w2 * __uint_as_float(pm[1][1]) + w3 * __uint_as_float(pm[2][1]));
  u32x2 ow[4];
#pragma unroll
  for (int j = 0; j < 4; ++j) {
    float r[4];
#pragma unroll
    for (int e = 0; e < 4; ++e) {
      const int w = (4 * j + e) >> 1;
      const unsigned a1 = pv[0][w >> 2][w & 3], a2 = pv[1][w >> 2][w & 3], a3 = pv[2][w >> 2][w & 3];
      const float f1 = (e & 1) ? bfhi(a1) : bflo(a1), f2 = (e & 1) ? bfhi(a2) : bflo(a2), f3 = (e & 1) ? bfhi(a3) : bflo(a3);
      r[e] = (w0 * own[4 * j + e] + w1 * f1 + w2 * f2 + w3 * f3) * inv;
    }
    ow[j] = u32x2{cvtpk(r[0], r[1]), cvtpk(r[2], r[3])};
  }
  const __amdgpu_buffer_rsrc_t ro = rsrc(out + (size_t)pair * ROWS * DH, ROWS * DH * 2);
  const int oo = (cw * 32 + c) * (DH * 2) + split * 64 + hh * 16;
#pragma unroll
  for (int j = 0; j < 4; j += 2) {
    const auto x = __builtin_amdgcn_permlane32_swap(ow[j][0], ow[j + 1][0], false, false);
    const auto y = __builtin_amdgcn_permlane32_swap(ow[j][1], ow[j + 1][1], false, false);
    __builtin_amdgcn_raw_buffer_store_b128(u32x4{x[0], y[0], x[1], y[1]}, ro, oo + j * 16, 0, OUT_AUX);
  }
}

extern "C" void kernel_launch(void* const* d_in, const int* in_sizes, int n_in, void* d_out, int out_size, void* d_ws, size_t ws_size,
                              hipStream_t stream) {
  hipLaunchKernelGGL(attn_split, dim3(NPAIR * NSPLIT), dim3(512), 0, stream, (const uint16_t*)d_in[0], (const int*)d_in[1], (const int*)d_in[2],
                     (const uint16_t*)d_in[3], (const uint16_t*)d_in[4], (const uint16_t*)d_in[5], (const uint16_t*)d_in[6], (char*)d_ws, (uint16_t*)d_out);
}
```
